```python
import torch

def _softmax(x, axis=-1):
    return torch.softmax(x, dim=axis)
def _gelu(x):
    return torch.nn.functional.gelu(x, approximate="tanh")
def _silu(x):
    return torch.nn.functional.silu(x)
def _sigmoid(x):
    return torch.sigmoid(x)
def _layernorm(x, axis=-1, eps=1e-5):
    m = x.mean(dim=axis, keepdim=True)
    v = x.var(dim=axis, keepdim=True, unbiased=False)
    return (x - m) / torch.sqrt(v + eps)
def _scatter_add(tgt, idx, src, axis=0):
    return tgt.clone().index_add_(axis, idx, src)
def _segment_sum(src, seg, n):
    out = torch.zeros((n,) + src.shape[1:], dtype=src.dtype)
    return out.index_add_(0, seg, src)
def _pad(x, pw):
    flat = tuple(v for pair in reversed(pw) for v in pair)
    return torch.nn.functional.pad(x, flat)

kernel_name = 'parallel_sum__compressed_kv_cla__dense_activation_gelu__prompt'

def setup_inputs():

    _w = (lambda a: (lambda t: t.to(torch.bfloat16) if t.is_floating_point() else t)(torch.as_tensor(a)))
    def _A_setup():
        B, H, Sq, Sk, D, M = 1, 32, 2048, 2048, 128, 512
        Dc = 4 * D
        return {
            "x": _w((lambda *s: torch.randn(*s, dtype=torch.bfloat16) * 0.05)(B, Sq, M)),
            "c_kv": _w((lambda *s: torch.randn(*s, dtype=torch.bfloat16) * 0.4)(B, Sk, Dc)),
            "w_q": _w((lambda *s: torch.randn(*s, dtype=torch.bfloat16) * 3.2)(M, H, D) * (M ** -0.5)),
            "w_uk": _w((lambda *s: torch.randn(*s, dtype=torch.bfloat16) * 3.2)(Dc, H, D) * (Dc ** -0.5)),
            "w_uv": _w((lambda *s: torch.randn(*s, dtype=torch.bfloat16) * 0.4)(Dc, H, D) * (Dc ** -0.5)),
            "w_o": _w((lambda *s: torch.randn(*s, dtype=torch.bfloat16) * 0.4)(H, D, M) * ((H * D) ** -0.5)),
        }
    _A_in = _A_setup()

    def _B_setup():
        B, S, D, F = 1, 2048, 512, 1376
        return {
            "x": _w((lambda *s: torch.randn(*s, dtype=torch.bfloat16) * 0.05)(B, S, D)),
            "w1": _w((lambda *s: torch.randn(*s, dtype=torch.bfloat16) * 0.4)(D, F) * (D ** -0.5)),
            "b1": _w((lambda *s: torch.randn(*s, dtype=torch.bfloat16) * 12.8)(F) * 0.01),
            "w2": _w((lambda *s: torch.randn(*s, dtype=torch.bfloat16) * 0.2)(F, D) * (F ** -0.5)),
            "b2": _w((lambda *s: torch.randn(*s, dtype=torch.bfloat16) * 3.2)(D) * 0.01),
        }
    _B_in = _B_setup()

    return {
        "A_x": _A_in["x"],
        "A_c_kv": _A_in["c_kv"],
        "A_w_q": _A_in["w_q"],
        "A_w_uk": _A_in["w_uk"],
        "A_w_uv": _A_in["w_uv"],
        "A_w_o": _A_in["w_o"],
        "B_x": _B_in["x"],
        "B_w1": _B_in["w1"],
        "B_b1": _B_in["b1"],
        "B_w2": _B_in["w2"],
        "B_b2": _B_in["b2"],
    }

def reference(A_x, A_c_kv, A_w_q, A_w_uk, A_w_uv, A_w_o, B_x, B_w1, B_b1, B_w2, B_b2):
    def _A_ref(x, c_kv, w_q, w_uk, w_uv, w_o):
        B, Sq, M = x.shape[0], x.shape[1], x.shape[2]
        Sk = c_kv.shape[1]
        H, D = w_q.shape[1], w_q.shape[2]
        q = torch.einsum("bqm,mhd->bhqd", x, w_q)
        k = torch.einsum("bkc,chd->bhkd", c_kv, w_uk)
        v = torch.einsum("bkc,chd->bhkd", c_kv, w_uv)
        scores = torch.einsum("bhqd,bhkd->bhqk", q, k) * (D ** -0.5)
        off = Sk - Sq
        qi = torch.arange(Sq)[:, None]
        kj = torch.arange(Sk)[None, :]
        cmask = kj <= (qi + off)
        scores = torch.where(cmask[None, None], scores, -1e9)
        p = _softmax(scores, axis=-1)
        ctx = torch.einsum("bhqk,bhkd->bhqd", p, v)
        return torch.einsum("bhqd,hdm->bqm", ctx, w_o)

    def _B_ref(x, w1, b1, w2, b2):
        h = torch.einsum("bsd,df->bsf", x, w1) + b1
        h = _gelu(h)
        return torch.einsum("bsf,fd->bsd", h, w2) + b2

    x = A_x
    xn = _layernorm(x, axis=-1)
    return x + _A_ref(xn, A_c_kv, A_w_q, A_w_uk, A_w_uv, A_w_o) + _B_ref(xn, B_w1, B_b1, B_w2, B_b2)
```

```cpp
#include <hip/hip_runtime.h>
#include <hip/hip_bf16.h>
#include <stdint.h>
#define ABL 0

typedef unsigned short u16;
typedef short bf16x8 __attribute__((ext_vector_type(8)));
typedef short s16x4 __attribute__((ext_vector_type(4)));
typedef float f32x4 __attribute__((ext_vector_type(4)));
typedef float f32x16 __attribute__((ext_vector_type(16)));
typedef unsigned u32x4 __attribute__((ext_vector_type(4)));
typedef unsigned u32x2 __attribute__((ext_vector_type(2)));
#define LAS __attribute__((address_space(3)))

constexpr int S_LEN = 2048, DM = 512, NH = 32, HD = 128, NQKV = 4096, FF = 1376, KT_TOT = 86  , AHLD = 5504;
constexpr size_t WS_XN = 0;
constexpr size_t WS_W1P = 2u << 20;
constexpr size_t WS_Q = 8u << 20;
constexpr size_t WS_K = 24u << 20;
constexpr size_t WS_V = 40u << 20;
constexpr size_t WS_AH = 56u << 20;
constexpr size_t WS_PART = 80u << 20;

__device__ __forceinline__ unsigned cvt_pk_bf16(float lo, float hi) {
    unsigned r; asm volatile("v_cvt_pk_bf16_f32 %0, %1, %2" : "=v"(r) : "v"(lo), "v"(hi)); return r;
}
__device__ __forceinline__ float bf2f(u16 v) { return __uint_as_float(((unsigned)v) << 16); }

__global__ __launch_bounds__(256) void k_prep(const u16* __restrict__ x, const u16* __restrict__ w1, u16* __restrict__ xn,
                                              u16* __restrict__ w1p, u16* __restrict__ ah) {
    const int wv = blockIdx.x * 4 + (threadIdx.x >> 6), lane = threadIdx.x & 63;
    if (wv < S_LEN) {
        const u32x4 raw = *(const u32x4*)(x + (size_t)wv * DM + lane * 8);
        float v[8];
#pragma unroll
        for (int i = 0; i < 4; ++i) { v[2 * i] = __uint_as_float(raw[i] << 16); v[2 * i + 1] = __uint_as_float(raw[i] & 0xffff0000u); }
        float s = 0.f;
#pragma unroll
        for (int i = 0; i < 8; ++i) s += v[i];
#pragma unroll
        for (int o = 32; o >= 1; o >>= 1) s += __shfl_xor(s, o);
        const float mean = s * (1.0f / DM);
        float q = 0.f;
#pragma unroll
        for (int i = 0; i < 8; ++i) { const float d = v[i] - mean; q += d * d; }
#pragma unroll
        for (int o = 32; o >= 1; o >>= 1) q += __shfl_xor(q, o);
        const float den = sqrtf(q * (1.0f / DM) + 1e-5f);
        u32x4 o4;
#pragma unroll
        for (int i = 0; i < 4; ++i) o4[i] = cvt_pk_bf16((v[2 * i] - mean) / den, (v[2 * i + 1] - mean) / den);
        *(u32x4*)(xn + (size_t)wv * DM + lane * 8) = o4;
        if (lane < 4) *(u32x4*)(ah + ((size_t)((NQKV + FF) >> 5) * S_LEN + wv) * 32 + lane * 8) = (u32x4){0u, 0u, 0u, 0u};
    } else if (wv < S_LEN + DM) {
        const int r = wv - S_LEN;
        for (int c = lane; c < FF / 8; c += 64) *(u32x4*)(w1p + (size_t)r * NQKV + c * 8) = *(const u32x4*)(w1 + (size_t)r * FF + c * 8);
    }
}

namespace g1 {
constexpr int BM = 256, BK = 64, HALF = 128, HTB = HALF * BK * 2, STAGE_BYTES = 8 * HTB;
#define G1_NUNITS 432
constexpr int LDA = 512, LDB = 4096, NUNITS = G1_NUNITS, NXCD = 8, UPX = NUNITS / NXCD;
__device__ __forceinline__ int lds_byte(int r, int c) { const int st = (r >> 4) * 2 + (c >> 5), rr = r & 15, cc = c & 31, ob = rr * 64 + cc * 2; return st * 1024 + (ob ^ (((ob >> 9) & 1) << 5)); }
__device__ __forceinline__ void stage_rc(int b, int& R, int& C) { const int st = b / 1024, sb = b % 1024, swz = sb ^ (((sb >> 9) & 1) << 5); R = (st >> 1) * 16 + swz / 64; C = (st & 1) * 32 + (swz % 64) / 2; }

struct Ptrs { const u16* xn; const u16* ckv; const u16* wq; const u16* wuk; const u16* wuv; const u16* w1p; const u16* b1; u16* q; u16* k; u16* v; u16* ah; };
struct Unit { const u16* A; const u16* B; int pm; int pn; int seg; };
__device__ __forceinline__ bool unit_of(const Ptrs& P, int round, int c, Unit& u) {
    const int L = round * 256 + c; if (L >= NUNITS) return false;
    const int id = (L % NXCD) * UPX + (L / NXCD);
    if (id < 384) { u.seg = id >> 7; const int r = id & 127; u.pm = r & 7; u.pn = r >> 3;
        u.A = (u.seg == 2 ? P.xn : P.ckv) + (size_t)u.pm * BM * LDA;
        u.B = (u.seg == 0 ? P.wuk : (u.seg == 1 ? P.wuv : P.wq)) + u.pn * BM;
    } else { const int r = id - 384; u.seg = 3; u.pm = r & 7; u.pn = r >> 3;
        u.A = P.xn + (size_t)u.pm * BM * LDA; u.B = P.w1p + (u.pn < 5 ? u.pn * BM : 1120); }
    return true;
}
__device__ __forceinline__ u16 f2bf(float f) { return (u16)(cvt_pk_bf16(f, 0.f) & 0xffffu); }
__device__ __forceinline__ float gelu_tanh(float v) {
    const float u = 0.7978845608028654f * (v + 0.044715f * v * v * v);
    return v / (1.0f + __expf(-2.0f * u));
}

__global__ __launch_bounds__(512, 2) void k_gemm1(Ptrs P) {
    extern __shared__ __attribute__((aligned(16))) unsigned char smem[];
    LAS unsigned char* lds = (LAS unsigned char*)smem;
    const int tid = threadIdx.x, wid = __builtin_amdgcn_readfirstlane(tid >> 6), lane = tid & 63, wr = wid >> 2, wc = wid & 3, fr = lane & 15, fq = lane >> 4;
    const int nt = 512 / BK;
    unsigned voffA[2], voffB[2];
#pragma unroll
    for (int i = 0; i < 2; ++i) {
        int R, C; stage_rc(tid * 16 + i * 8192, R, C); voffA[i] = (unsigned)(R * LDA + C) * 2u;
        const int row = 4 * wid + 32 * i + (lane >> 4), pos = lane & 15, X = (row & 3) | (((row >> 3) & 1) << 2);
        voffB[i] = (unsigned)(row * LDB + 8 * (pos ^ (X << 1))) * 2u;
    }
    const size_t kstepA = (size_t)BK * 2, kstepB = (size_t)BK * LDB * 2, hstepA = (size_t)HALF * LDA * 2, hstepB = (size_t)HALF * 2;
    const unsigned ldsw = (unsigned)wid * 1024u;
    const int aoff = lds_byte(wr * 64 + fr, fq * 8);
    int boffn[2];
    { const int g = lane >> 4, q = (lane & 15) >> 2, p = lane & 3, X = q | ((g & 1) << 2);
#pragma unroll
      for (int n = 0; n < 2; ++n) boffn[n] = 256 * (8 * g + q) + 32 * (((2 * wc) ^ X) ^ n) + 8 * p; }
    unsigned bbase[2];
#pragma unroll
    for (int n = 0; n < 2; ++n) bbase[n] = (unsigned)(uintptr_t)lds + 65536u + (unsigned)boffn[n];
#define SA(b, h) (((b) * 2 + (h)) * HTB)
#define SB(b, h) ((4 + (b) * 2 + (h)) * HTB)
#define STAGE(bufoff, gbase, voff) do { _Pragma("unroll") for (int _i = 0; _i < 2; ++_i) \
        __builtin_amdgcn_global_load_lds((const unsigned*)((const char*)(gbase) + (voff)[_i]), (LAS unsigned*)(lds + (bufoff) + ldsw + _i * 8192), 16, 0, 0); } while (0)
#define LDA_(dst, b, h) do { _Pragma("unroll") for (int m = 0; m < 4; ++m) _Pragma("unroll") for (int k = 0; k < 2; ++k) dst[m][k] = *(const LAS bf16x8*)(lds + SA(b, h) + aoff + m * 2048 + k * 1024); } while (0)
#define TRD(dst, base, off) asm volatile("ds_read_b64_tr_b16 %0, %1 offset:%2" : "=v"(dst) : "v"(base), "i"(off))
#define LDB_(dst, b, h) do { s16x4 l_[2][2], h_[2][2]; \
        _Pragma("unroll") for (int n = 0; n < 2; ++n) _Pragma("unroll") for (int k = 0; k < 2; ++k) { \
            TRD(l_[n][k], bbase[n], SB(b, h) - 65536 + k * 8192); TRD(h_[n][k], bbase[n], SB(b, h) - 65536 + k * 8192 + 1024); } \
        _Pragma("unroll") for (int n = 0; n < 2; ++n) _Pragma("unroll") for (int k = 0; k < 2; ++k) \
            dst[n][k] = (bf16x8){l_[n][k][0], l_[n][k][1], l_[n][k][2], l_[n][k][3], h_[n][k][0], h_[n][k][1], h_[n][k][2], h_[n][k][3]}; } while (0)
#define MMA(ai, bj, At, Bt) do { __builtin_amdgcn_s_setprio(1); _Pragma("unroll") for (int m = 0; m < 4; ++m) _Pragma("unroll") for (int n = 0; n < 2; ++n) _Pragma("unroll") for (int k = 0; k < 2; ++k) \
        acc[ai][bj][m][n] = __builtin_amdgcn_mfma_f32_16x16x32_bf16(Bt[n][k], At[m][k], acc[ai][bj][m][n], 0, 0, 0); __builtin_amdgcn_s_setprio(0); } while (0)
#define WAIT_V(n) asm volatile("s_waitcnt vmcnt(" #n ")" ::: "memory")
#define WAIT_L(n) asm volatile("s_waitcnt lgkmcnt(" #n ")" ::: "memory")
#define BAR __builtin_amdgcn_s_barrier()
#define SCHED __builtin_amdgcn_sched_barrier(0)
    Unit cur, nxt;
    if (!unit_of(P, 0, blockIdx.x, cur)) return;
    f32x4 acc[2][2][4][2];
#pragma unroll
    for (int a = 0; a < 2; ++a)
#pragma unroll
        for (int b = 0; b < 2; ++b)
#pragma unroll
            for (int m = 0; m < 4; ++m)
#pragma unroll
                for (int n = 0; n < 2; ++n) acc[a][b][m][n] = (f32x4){0.f, 0.f, 0.f, 0.f};
    bf16x8 At[4][2], B0[2][2], B1[2][2];
    const char* cA = (const char*)cur.A; const char* cB = (const char*)cur.B;
    STAGE(SB(0, 0), cB, voffB); STAGE(SA(0, 0), cA, voffA); STAGE(SB(0, 1), cB + hstepB, voffB); STAGE(SA(0, 1), cA + hstepA, voffA);
    if (wr == 1) BAR;
    WAIT_V(4); BAR;
    STAGE(SB(1, 0), cB + kstepB, voffB); STAGE(SA(1, 0), cA + kstepA, voffA); STAGE(SB(1, 1), cB + hstepB + kstepB, voffB);
    WAIT_V(6); BAR;
    int round = 0;
    for (;;) {
        const bool has_next = unit_of(P, round + 1, blockIdx.x, nxt);
        const char* nA = has_next ? (const char*)nxt.A : cA; const char* nB = has_next ? (const char*)nxt.B : cB;
        for (int t = 0; t < nt; t += 2) {
            const bool last = (t == nt - 2);
            const char* a1 = cA + (size_t)(t + 1) * kstepA;
            const char* a2 = last ? nA : cA + (size_t)(t + 2) * kstepA; const char* b2 = last ? nB : cB + (size_t)(t + 2) * kstepB;
            const char* a3 = a2 + kstepA; const char* b3 = b2 + kstepB;
            LDB_(B0, 0, 0); SCHED; LDA_(At, 0, 0); STAGE(SA(1, 1), a1 + hstepA, voffA);
            WAIT_L(8); BAR; WAIT_L(0); SCHED; MMA(0, 0, At, B0); BAR; SCHED;
            LDB_(B1, 0, 1); STAGE(SB(0, 0), b2, voffB);
            BAR; WAIT_L(0); SCHED; MMA(0, 1, At, B1); BAR;
            LDA_(At, 0, 1); STAGE(SA(0, 0), a2, voffA);
            BAR; WAIT_L(0); SCHED; MMA(1, 0, At, B0); BAR; SCHED;
            STAGE(SB(0, 1), b2 + hstepB, voffB);
            WAIT_V(6); BAR; MMA(1, 1, At, B1); BAR;
            LDB_(B0, 1, 0); SCHED; LDA_(At, 1, 0); STAGE(SA(0, 1), a2 + hstepA, voffA);
            WAIT_L(8); BAR; WAIT_L(0); SCHED; MMA(0, 0, At, B0); BAR; SCHED;
            LDB_(B1, 1, 1); STAGE(SB(1, 0), b3, voffB);
            BAR; WAIT_L(0); SCHED; MMA(0, 1, At, B1); BAR;
            LDA_(At, 1, 1); STAGE(SA(1, 0), a3, voffA);
            BAR; WAIT_L(0); SCHED; MMA(1, 0, At, B0); BAR; SCHED;
            STAGE(SB(1, 1), b3 + hstepB, voffB);
            WAIT_V(6); BAR; MMA(1, 1, At, B1); BAR;
        }
        {
            const int row0 = cur.pm * BM + wr * 64 + fr;
            if (cur.seg < 3 || ABL == 1) {
                u16* base = (cur.seg == 0 ? P.k : (cur.seg == 1 ? P.v : P.q));
#pragma unroll
                for (int bj = 0; bj < 2; ++bj) {
                    u16* hb = base + ((size_t)((cur.pn * 2 + bj) * 4 + wc) * S_LEN) * 32 + fq * 8;
#pragma unroll
                    for (int ai = 0; ai < 2; ++ai)
#pragma unroll
                        for (int m = 0; m < 4; ++m) {
                            const f32x4 v0 = acc[ai][bj][m][0], v1 = acc[ai][bj][m][1];
                            u32x4 w; w.x = cvt_pk_bf16(v0[0], v0[1]); w.y = cvt_pk_bf16(v0[2], v0[3]); w.z = cvt_pk_bf16(v1[0], v1[1]); w.w = cvt_pk_bf16(v1[2], v1[3]);
                            *(u32x4*)(hb + (size_t)(row0 + ai * HALF + m * 16) * 32) = w;
                        }
                }
            } else {
                const int n0 = cur.pn < 5 ? cur.pn * BM : 1120;
#pragma unroll
                for (int bj = 0; bj < 2; ++bj) {
                    const int cb = n0 + bj * HALF + wc * 32;
                    f32x4 bv[2];
#pragma unroll
                    for (int n = 0; n < 2; ++n) { const u32x2 braw = *(const u32x2*)(P.b1 + cb + 16 * n + 4 * fq);
                        bv[n] = (f32x4){__uint_as_float(braw.x << 16), __uint_as_float(braw.x & 0xffff0000u), __uint_as_float(braw.y << 16), __uint_as_float(braw.y & 0xffff0000u)}; }
                    u16* ob = P.ah + ((size_t)((NQKV + cb) >> 5) * S_LEN) * 32 + fq * 8;
#pragma unroll
                    for (int ai = 0; ai < 2; ++ai)
#pragma unroll
                        for (int m = 0; m < 4; ++m) {
                            f32x4 v0 = acc[ai][bj][m][0] + bv[0], v1 = acc[ai][bj][m][1] + bv[1];
#pragma unroll
                            for (int j = 0; j < 4; ++j) { v0[j] = gelu_tanh(v0[j]); v1[j] = gelu_tanh(v1[j]); }
                            u32x4 w; w.x = cvt_pk_bf16(v0[0], v0[1]); w.y = cvt_pk_bf16(v0[2], v0[3]); w.z = cvt_pk_bf16(v1[0], v1[1]); w.w = cvt_pk_bf16(v1[2], v1[3]);
                            *(u32x4*)(ob + (size_t)(row0 + ai * HALF + m * 16) * 32) = w;
                        }
                }
            }
        }
        if (!has_next) break;
#pragma unroll
        for (int a = 0; a < 2; ++a)
#pragma unroll
            for (int b = 0; b < 2; ++b)
#pragma unroll
                for (int m = 0; m < 4; ++m)
#pragma unroll
                    for (int n = 0; n < 2; ++n) acc[a][b][m][n] = (f32x4){0.f, 0.f, 0.f, 0.f};
        cur = nxt; cA = nA; cB = nB; ++round;
    }
    WAIT_V(0);
    if (wr == 0) BAR;
#undef SA
#undef SB
#undef STAGE
#undef LDA_
#undef TRD
#undef LDB_
#undef MMA
#undef WAIT_V
#undef WAIT_L
#undef BAR
#undef SCHED
}
}

namespace at {
constexpr int KVB = 64, SHM_K = KVB * HD * 2, SHM_V = KVB * HD * 2;
constexpr int LDS_BYTES = 2 * SHM_K + 2 * SHM_V;
constexpr float C2 = 0.08838834764831845f * 1.4426950408889634f;
__device__ __forceinline__ int v_st(int key, int d) { return ((key >> 3) * 4 + (d >> 5)) * 512 + (key & 7) * 64 + (d & 31) * 2; }

__global__ __launch_bounds__(512, 1) void k_attn(const u16* __restrict__ Qb, const u16* __restrict__ Kb, const u16* __restrict__ Vb, u16* __restrict__ ah) {
    extern __shared__ __attribute__((aligned(16))) unsigned char smem[];
    LAS unsigned char* lds = (LAS unsigned char*)smem;
    const int tid = threadIdx.x, wid = __builtin_amdgcn_readfirstlane(tid >> 6), lane = tid & 63, r32 = lane & 31, hi = lane >> 5;
    const int b = blockIdx.x, h = (b & 7) + 8 * ((b >> 3) & 3), x = b >> 5;
    const int blk = (wid >> 2) ? 15 - x : x, R0 = 128 * blk + 32 * (wid & 3);
    const int tdiag = R0 >> 6, NTw = tdiag + 1, NT = 32 - 2 * x;
    const int srow = (tid >> 2) & 63, sq4 = tid & 3, sdb = tid >> 8;
    const size_t gofs0 = ((size_t)(h * 4 + sdb) * S_LEN + srow) * 32 + sq4 * 8, gofs1 = gofs0 + (size_t)2 * S_LEN * 32;
    const u16* Kh = Kb + gofs0; const u16* Vh = Vb + gofs0; const size_t gd2 = gofs1 - gofs0;
    const int kw0 = srow * 256 + (((sdb * 4 + sq4) ^ (srow & 15)) << 4), kw1 = srow * 256 + ((((sdb + 2) * 4 + sq4) ^ (srow & 15)) << 4);
    const int vw0 = v_st(srow, 32 * sdb + 8 * sq4), vw1 = v_st(srow, 32 * (sdb + 2) + 8 * sq4);
    bf16x8 qr[8];
#pragma unroll
    for (int s = 0; s < 8; ++s) qr[s] = *(const bf16x8*)(Qb + ((size_t)(h * 4 + (s >> 1)) * S_LEN + R0 + r32) * 32 + 16 * (s & 1) + 8 * hi);
    const int ka0 = r32 * 256 + ((hi ^ (r32 & 15)) << 4);
    const int vb0 = 64 * (4 * hi + ((lane & 15) >> 2)) + 32 * ((lane >> 4) & 1) + 8 * (lane & 3);
    f32x16 o[4] = {};
    float m_run = -1e30f, l_run = 0.f;
    bf16x8 sk0, sk1, sv0, sv1;
    sk0 = *(const bf16x8*)(Kh); sk1 = *(const bf16x8*)(Kh + gd2);
    sv0 = *(const bf16x8*)(Vh); sv1 = *(const bf16x8*)(Vh + gd2);
    *(LAS bf16x8*)(lds + kw0) = sk0; *(LAS bf16x8*)(lds + kw1) = sk1;
    *(LAS bf16x8*)(lds + 2 * SHM_K + vw0) = sv0; *(LAS bf16x8*)(lds + 2 * SHM_K + vw1) = sv1;
    __syncthreads();
    for (int t = 0; t < NT; ++t) {
        const int cb = t & 1, nb = cb ^ 1;
        const bool more = t + 1 < NT;
        if (more) {
            const u16* kp = Kh + (size_t)(t + 1) * KVB * 32; const u16* vp = Vh + (size_t)(t + 1) * KVB * 32;
            sk0 = *(const bf16x8*)(kp); sk1 = *(const bf16x8*)(kp + gd2);
            sv0 = *(const bf16x8*)(vp); sv1 = *(const bf16x8*)(vp + gd2);
        }
        if (t < NTw) {
            LAS unsigned char* kb = lds + cb * SHM_K; LAS unsigned char* vbuf = lds + 2 * SHM_K + cb * SHM_V;
            f32x16 p0 = {}, p1 = {};
#pragma unroll
            for (int s = 0; s < 8; ++s) {
                const int a = ka0 ^ (s << 5);
                const bf16x8 k0 = *(const LAS bf16x8*)(kb + a), k1 = *(const LAS bf16x8*)(kb + a + 32 * 256);
                p0 = __builtin_amdgcn_mfma_f32_32x32x16_bf16(k0, qr[s], p0, 0, 0, 0);
                p1 = __builtin_amdgcn_mfma_f32_32x32x16_bf16(k1, qr[s], p1, 0, 0, 0);
            }
            if (t == tdiag) {
                const int dq = R0 + r32 - 64 * t - 4 * hi;
#pragma unroll
                for (int r = 0; r < 16; ++r) { const int c = (r & 3) + 8 * (r >> 2);
                    if (dq - c < 0) p0[r] = -__builtin_inff();
                    if (dq - c - 32 < 0) p1[r] = -__builtin_inff(); }
            }
            float pmax = fmaxf(p0[0], p1[0]);
#pragma unroll
            for (int r = 1; r < 16; ++r) pmax = fmaxf(pmax, fmaxf(p0[r], p1[r]));
            { auto rr = __builtin_amdgcn_permlane32_swap(__float_as_uint(pmax), __float_as_uint(pmax), false, false);
              pmax = fmaxf(__uint_as_float(rr[0]), __uint_as_float(rr[1])); }
            const float mn = fmaxf(m_run, pmax), alpha = __builtin_amdgcn_exp2f((m_run - mn) * C2); m_run = mn;
            const float mnL = -mn * C2;
            float ps = 0.f;
#pragma unroll
            for (int r = 0; r < 16; ++r) { p0[r] = __builtin_amdgcn_exp2f(fmaf(p0[r], C2, mnL)); p1[r] = __builtin_amdgcn_exp2f(fmaf(p1[r], C2, mnL)); ps += p0[r] + p1[r]; }
            l_run = l_run * alpha + ps;
            if (__any(alpha < 1.f)) {
#pragma unroll
                for (int d = 0; d < 4; ++d)
#pragma unroll
                    for (int r = 0; r < 16; ++r) o[d][r] *= alpha;
            }
            bf16x8 pa[4];
#pragma unroll
            for (int s2 = 0; s2 < 2; ++s2) {
                u32x4 w0 = {cvt_pk_bf16(p0[8 * s2 + 0], p0[8 * s2 + 1]), cvt_pk_bf16(p0[8 * s2 + 2], p0[8 * s2 + 3]), cvt_pk_bf16(p0[8 * s2 + 4], p0[8 * s2 + 5]), cvt_pk_bf16(p0[8 * s2 + 6], p0[8 * s2 + 7])};
                u32x4 w1 = {cvt_pk_bf16(p1[8 * s2 + 0], p1[8 * s2 + 1]), cvt_pk_bf16(p1[8 * s2 + 2], p1[8 * s2 + 3]), cvt_pk_bf16(p1[8 * s2 + 4], p1[8 * s2 + 5]), cvt_pk_bf16(p1[8 * s2 + 6], p1[8 * s2 + 7])};
                pa[s2] = *(bf16x8*)&w0; pa[2 + s2] = *(bf16x8*)&w1;
            }
#pragma unroll
            for (int S = 0; S < 4; ++S)
#pragma unroll
                for (int d0 = 0; d0 < 4; ++d0) {
                    const s16x4 lo = __builtin_amdgcn_ds_read_tr16_b64_v4i16((LAS s16x4*)(vbuf + vb0 + 4096 * S + 512 * d0));
                    const s16x4 hv = __builtin_amdgcn_ds_read_tr16_b64_v4i16((LAS s16x4*)(vbuf + vb0 + 4096 * S + 512 * d0 + 2048));
                    o[d0] = __builtin_amdgcn_mfma_f32_32x32x16_bf16((bf16x8){lo[0], lo[1], lo[2], lo[3], hv[0], hv[1], hv[2], hv[3]}, pa[S], o[d0], 0, 0, 0);
                }
            if (t == NTw - 1) {
                auto rr = __builtin_amdgcn_permlane32_swap(__float_as_uint(l_run), __float_as_uint(l_run), false, false);
                const float inv = 1.0f / (__uint_as_float(rr[0]) + __uint_as_float(rr[1]));
                u16* orow = ah + ((size_t)(h * 4) * S_LEN + R0 + r32) * 32 + 4 * hi;
#pragma unroll
                for (int d0 = 0; d0 < 4; ++d0)
#pragma unroll
                    for (int g = 0; g < 4; ++g) {
                        const u32x2 w = {cvt_pk_bf16(o[d0][4 * g] * inv, o[d0][4 * g + 1] * inv), cvt_pk_bf16(o[d0][4 * g + 2] * inv, o[d0][4 * g + 3] * inv)};
                        *(u32x2*)(orow + (size_t)d0 * S_LEN * 32 + 8 * g) = w;
                    }
            }
        }
        if (more) {
            *(LAS bf16x8*)(lds + nb * SHM_K + kw0) = sk0; *(LAS bf16x8*)(lds + nb * SHM_K + kw1) = sk1;
            *(LAS bf16x8*)(lds + 2 * SHM_K + nb * SHM_V + vw0) = sv0; *(LAS bf16x8*)(lds + 2 * SHM_K + nb * SHM_V + vw1) = sv1;
        }
        __syncthreads();
    }
}
}

namespace g2 {
constexpr int STG = 32768, LDS_BYTES = 4 * STG;
__global__ __launch_bounds__(512, 2) void k_gemm2(const u16* __restrict__ ah, const u16* __restrict__ wo, const u16* __restrict__ w2, float* __restrict__ part) {
    extern __shared__ __attribute__((aligned(16))) unsigned char smem[];
    LAS unsigned char* lds = (LAS unsigned char*)smem;
    const int tid = threadIdx.x, wid = __builtin_amdgcn_readfirstlane(tid >> 6), lane = tid & 63, fr = lane & 15, fq = lane >> 4;
    const int b = blockIdx.x, xcd = b & 7, sp = xcd >> 1, pm = (xcd & 1) * 8 + ((b >> 3) & 7), pn = (b >> 6) & 3;
    const int kt0 = sp < 2 ? 22 * sp : 44 + 21 * (sp - 2), nkt = sp < 2 ? 22 : 21, nit = (nkt + 1) >> 1;
    const int grp = wid >> 2, qm = (wid >> 1) & 1, qn = wid & 1;
    unsigned voffA[2], voffB[2], voffBc;
#pragma unroll
    for (int i = 0; i < 2; ++i) {
        int R, C; g1::stage_rc(tid * 16 + i * 8192, R, C);
        voffA[i] = (unsigned)(((C >> 5) * S_LEN + R) * 32 + (C & 31)) * 2u;
        const int r = 4 * wid + 32 * i + (lane >> 4), pos = lane & 15, X = (r & 3) | (((r >> 3) & 1) << 2), ch = pos ^ (X << 1);
        const int p5 = r & 31, rr = (r & 32) | (16 * ((p5 >> 2) & 1) + 4 * (p5 >> 3) + (p5 & 3));
        voffB[i] = (unsigned)(rr * DM + 8 * ch) * 2u;
        if (i == 1) voffBc = (unsigned)(31 * DM + 8 * ch) * 2u;
    }
    const unsigned ldsw = (unsigned)wid * 1024u;
    const int aoff = g1::lds_byte(qm * 64 + fr, fq * 8);
    unsigned bbase[4];
    { const int g = lane >> 4, q = (lane & 15) >> 2, p = lane & 3, X = q | ((g & 1) << 2);
#pragma unroll
      for (int n = 0; n < 4; ++n) bbase[n] = (unsigned)(uintptr_t)lds + (unsigned)(256 * (8 * g + q) + 32 * (((4 * qn) ^ X) ^ n) + 8 * p); }
    const char* Ab = (const char*)ah + (size_t)pm * 128 * 64;
    const char* Bcol = (const char*)0 + (size_t)pn * 256;
#define G2_STAGE(kt, st) do { const int kt_ = (kt); \
        const char* a_ = Ab + (size_t)kt_ * (2 * S_LEN * 64); \
        const char* b_ = (kt_ < 64 ? (const char*)wo + (size_t)kt_ * 64 * DM * 2 : (const char*)w2 + (size_t)(kt_ - 64) * 64 * DM * 2) + (size_t)Bcol; \
        _Pragma("unroll") for (int _i = 0; _i < 2; ++_i) \
            __builtin_amdgcn_global_load_lds((const unsigned*)(a_ + voffA[_i]), (LAS unsigned*)(lds + (st) * STG + ldsw + _i * 8192), 16, 0, 0); \
        __builtin_amdgcn_global_load_lds((const unsigned*)(b_ + voffB[0]), (LAS unsigned*)(lds + (st) * STG + 16384 + ldsw), 16, 0, 0); \
        __builtin_amdgcn_global_load_lds((const unsigned*)(b_ + (kt_ == KT_TOT - 1 ? voffBc : voffB[1])), (LAS unsigned*)(lds + (st) * STG + 16384 + ldsw + 8192), 16, 0, 0); } while (0)
    f32x4 acc[4][4];
#pragma unroll
    for (int m = 0; m < 4; ++m)
#pragma unroll
        for (int n = 0; n < 4; ++n) acc[m][n] = (f32x4){0.f, 0.f, 0.f, 0.f};
    G2_STAGE(kt0, 0); if (nkt > 1) G2_STAGE(kt0 + 1, 1);
    for (int it = 0; it < nit; ++it) {
        asm volatile("s_waitcnt vmcnt(0)" ::: "memory"); __builtin_amdgcn_s_barrier();
        const int sb = (it & 1) * 2;
        if (it + 1 < nit) { const int kn = kt0 + 2 * (it + 1); G2_STAGE(kn, sb ^ 2); if (2 * (it + 1) + 1 < nkt) G2_STAGE(kn + 1, (sb ^ 2) + 1); }
        if (2 * it + grp < nkt) {
            const int st = sb + grp;
            bf16x8 At[4][2]; s16x4 bl[4][2], bh[4][2];
#pragma unroll
            for (int n = 0; n < 4; ++n)
#pragma unroll
                for (int k = 0; k < 2; ++k) {
                    asm volatile("ds_read_b64_tr_b16 %0, %1 offset:%2" : "=v"(bl[n][k]) : "v"(bbase[n] + (unsigned)(st * STG)), "i"(16384 + k * 8192));
                    asm volatile("ds_read_b64_tr_b16 %0, %1 offset:%2" : "=v"(bh[n][k]) : "v"(bbase[n] + (unsigned)(st * STG)), "i"(16384 + k * 8192 + 1024));
                }
#pragma unroll
            for (int m = 0; m < 4; ++m)
#pragma unroll
                for (int k = 0; k < 2; ++k) At[m][k] = *(const LAS bf16x8*)(lds + st * STG + aoff + m * 2048 + k * 1024);
            asm volatile("s_waitcnt lgkmcnt(0)" ::: "memory"); __builtin_amdgcn_sched_barrier(0);
#pragma unroll
            for (int k = 0; k < 2; ++k)
#pragma unroll
                for (int m = 0; m < 4; ++m)
#pragma unroll
                    for (int n = 0; n < 4; ++n) {
                        const bf16x8 Bf = (bf16x8){bl[n][k][0], bl[n][k][1], bl[n][k][2], bl[n][k][3], bh[n][k][0], bh[n][k][1], bh[n][k][2], bh[n][k][3]};
                        acc[m][n] = __builtin_amdgcn_mfma_f32_16x16x32_bf16(Bf, At[m][k], acc[m][n], 0, 0, 0);
                    }
        }
    }
    __syncthreads();
    LAS f32x4* red = (LAS f32x4*)lds + (wid & 3) * 1024 + lane;
    if (grp == 1) {
#pragma unroll
        for (int m = 0; m < 4; ++m)
#pragma unroll
            for (int n = 0; n < 4; ++n) red[(m * 4 + n) * 64] = acc[m][n];
    }
    __syncthreads();
    if (grp == 0) {
        float* dst = part + ((((size_t)sp * 16 + pm) * 4 + pn) * 4 + (wid & 3)) * 4096 + lane * 4;
#pragma unroll
        for (int m = 0; m < 4; ++m)
#pragma unroll
            for (int n = 0; n < 4; ++n) *(f32x4*)(dst + (m * 4 + n) * 256) = acc[m][n] + red[(m * 4 + n) * 64];
    }
#undef G2_STAGE
}
__global__ __launch_bounds__(256) void k_reduce(const float* __restrict__ part, const u16* __restrict__ x, const u16* __restrict__ b2, u16* __restrict__ out) {
    const int gt = blockIdx.x * 256 + threadIdx.x;
    const int lane = gt & 63, mn = (gt >> 6) & 15, quad = (gt >> 10) & 3, pn = (gt >> 12) & 3, pm = gt >> 14;
    const size_t off = ((((size_t)pm) * 4 + pn) * 4 + quad) * 4096 + mn * 256 + lane * 4, sstr = (size_t)16 * 4 * 4 * 4096;
    f32x4 a = *(const f32x4*)(part + off) + *(const f32x4*)(part + off + sstr) + *(const f32x4*)(part + off + 2 * sstr) + *(const f32x4*)(part + off + 3 * sstr);
    const int row = pm * 128 + (quad >> 1) * 64 + (mn >> 2) * 16 + (lane & 15), col = pn * 128 + (quad & 1) * 64 + (mn & 3) * 16 + (lane >> 4) * 4;
    const u32x2 xr = *(const u32x2*)(x + (size_t)row * DM + col), br = *(const u32x2*)(b2 + col);
    const float o0 = a[0] + __uint_as_float(xr.x << 16) + __uint_as_float(br.x << 16), o1 = a[1] + __uint_as_float(xr.x & 0xffff0000u) + __uint_as_float(br.x & 0xffff0000u);
    const float o2 = a[2] + __uint_as_float(xr.y << 16) + __uint_as_float(br.y << 16), o3 = a[3] + __uint_as_float(xr.y & 0xffff0000u) + __uint_as_float(br.y & 0xffff0000u);
    *(u32x2*)(out + (size_t)row * DM + col) = (u32x2){cvt_pk_bf16(o0, o1), cvt_pk_bf16(o2, o3)};
}
}

extern "C" void kernel_launch(void* const* d_in, const int* in_sizes, int n_in, void* d_out, int out_size, void* d_ws, size_t ws_size,
                              hipStream_t stream) {
    const u16* x = (const u16*)d_in[0]; const u16* ckv = (const u16*)d_in[1];
    const u16* wq = (const u16*)d_in[2]; const u16* wuk = (const u16*)d_in[3]; const u16* wuv = (const u16*)d_in[4]; const u16* wo = (const u16*)d_in[5];
    const u16* w1 = (const u16*)d_in[7]; const u16* b1 = (const u16*)d_in[8]; const u16* w2 = (const u16*)d_in[9]; const u16* b2 = (const u16*)d_in[10];
    char* ws = (char*)d_ws;
    u16* xn = (u16*)(ws + WS_XN); u16* w1p = (u16*)(ws + WS_W1P); u16* qb = (u16*)(ws + WS_Q); u16* kb = (u16*)(ws + WS_K); u16* vb = (u16*)(ws + WS_V); u16* ah = (u16*)(ws + WS_AH);
    static bool attr_done = false;
    if (!attr_done) { attr_done = true;
        (void)hipFuncSetAttribute((const void*)g1::k_gemm1, hipFuncAttributeMaxDynamicSharedMemorySize, g1::STAGE_BYTES); }
    k_prep<<<dim3((S_LEN + DM) / 4), dim3(256), 0, stream>>>(x, w1, xn, w1p, ah);
    g1::Ptrs P{xn, ckv, wq, wuk, wuv, w1p, b1, qb, kb, vb, ah};
    g1::k_gemm1<<<dim3(256), dim3(512), g1::STAGE_BYTES, stream>>>(P);
    (void)hipFuncSetAttribute((const void*)at::k_attn, hipFuncAttributeMaxDynamicSharedMemorySize, at::LDS_BYTES);
    at::k_attn<<<dim3(256), dim3(512), at::LDS_BYTES, stream>>>(qb, kb, vb, ah);
    float* part = (float*)(ws + WS_PART);
    (void)hipFuncSetAttribute((const void*)g2::k_gemm2, hipFuncAttributeMaxDynamicSharedMemorySize, g2::LDS_BYTES);
    g2::k_gemm2<<<dim3(256), dim3(512), g2::LDS_BYTES, stream>>>(ah, wo, w2, part);
    g2::k_reduce<<<dim3(S_LEN * DM / 4 / 256), dim3(256), 0, stream>>>(part, x, b2, (u16*)d_out);
}
```

```cpp
#include <hip/hip_runtime.h>
#include <hip/hip_bf16.h>
#include <stdint.h>
#define ABL 0

typedef unsigned short u16;
typedef short bf16x8 __attribute__((ext_vector_type(8)));
typedef short s16x4 __attribute__((ext_vector_type(4)));
typedef float f32x4 __attribute__((ext_vector_type(4)));
typedef float f32x16 __attribute__((ext_vector_type(16)));
typedef unsigned u32x4 __attribute__((ext_vector_type(4)));
typedef unsigned u32x2 __attribute__((ext_vector_type(2)));
#define LAS __attribute__((address_space(3)))

constexpr int S_LEN = 2048, DM = 512, NH = 32, HD = 128, NQKV = 4096, FF = 1376, KT_TOT = 86  , AHLD = 5504;
constexpr size_t WS_XN = 0;
constexpr size_t WS_W1P = 2u << 20;
constexpr size_t WS_Q = 8u << 20;
constexpr size_t WS_K = 24u << 20;
constexpr size_t WS_V = 40u << 20;
constexpr size_t WS_AH = 56u << 20;
constexpr size_t WS_PART = 80u << 20;

__device__ __forceinline__ unsigned cvt_pk_bf16(float lo, float hi) {
    unsigned r; asm volatile("v_cvt_pk_bf16_f32 %0, %1, %2" : "=v"(r) : "v"(lo), "v"(hi)); return r;
}
__device__ __forceinline__ float bf2f(u16 v) { return __uint_as_float(((unsigned)v) << 16); }

__global__ __launch_bounds__(256) void k_prep(const u16* __restrict__ x, const u16* __restrict__ w1, u16* __restrict__ xn,
                                              u16* __restrict__ w1p, u16* __restrict__ ah) {
    const int wv = blockIdx.x * 4 + (threadIdx.x >> 6), lane = threadIdx.x & 63;
    if (wv < S_LEN) {
        const u32x4 raw = *(const u32x4*)(x + (size_t)wv * DM + lane * 8);
        float v[8];
#pragma unroll
        for (int i = 0; i < 4; ++i) { v[2 * i] = __uint_as_float(raw[i] << 16); v[2 * i + 1] = __uint_as_float(raw[i] & 0xffff0000u); }
        float s = 0.f;
#pragma unroll
        for (int i = 0; i < 8; ++i) s += v[i];
#pragma unroll
        for (int o = 32; o >= 1; o >>= 1) s += __shfl_xor(s, o);
        const float mean = s * (1.0f / DM);
        float q = 0.f;
#pragma unroll
        for (int i = 0; i < 8; ++i) { const float d = v[i] - mean; q += d * d; }
#pragma unroll
        for (int o = 32; o >= 1; o >>= 1) q += __shfl_xor(q, o);
        const float den = sqrtf(q * (1.0f / DM) + 1e-5f);
        u32x4 o4;
#pragma unroll
        for (int i = 0; i < 4; ++i) o4[i] = cvt_pk_bf16((v[2 * i] - mean) / den, (v[2 * i + 1] - mean) / den);
        *(u32x4*)(xn + (size_t)wv * DM + lane * 8) = o4;
        if (lane < 4) *(u32x4*)(ah + ((size_t)((NQKV + FF) >> 5) * S_LEN + wv) * 32 + lane * 8) = (u32x4){0u, 0u, 0u, 0u};
    } else if (wv < S_LEN + DM) {
        const int r = wv - S_LEN;
        for (int c = lane; c < FF / 8; c += 64) *(u32x4*)(w1p + (size_t)r * NQKV + c * 8) = *(const u32x4*)(w1 + (size_t)r * FF + c * 8);
    }
}

namespace g1 {
constexpr int BM = 256, BK = 64, HALF = 128, HTB = HALF * BK * 2, STAGE_BYTES = 8 * HTB;
#define G1_NUNITS 432
constexpr int LDA = 512, LDB = 4096, NUNITS = G1_NUNITS, NXCD = 8, UPX = NUNITS / NXCD;
__device__ __forceinline__ int lds_byte(int r, int c) { const int st = (r >> 4) * 2 + (c >> 5), rr = r & 15, cc = c & 31, ob = rr * 64 + cc * 2; return st * 1024 + (ob ^ (((ob >> 9) & 1) << 5)); }
__device__ __forceinline__ void stage_rc(int b, int& R, int& C) { const int st = b / 1024, sb = b % 1024, swz = sb ^ (((sb >> 9) & 1) << 5); R = (st >> 1) * 16 + swz / 64; C = (st & 1) * 32 + (swz % 64) / 2; }

struct Ptrs { const u16* xn; const u16* ckv; const u16* wq; const u16* wuk; const u16* wuv; const u16* w1p; const u16* b1; u16* q; u16* k; u16* v; u16* ah; };
struct Unit { const u16* A; const u16* B; int pm; int pn; int seg; };
__device__ __forceinline__ bool unit_of(const Ptrs& P, int round, int c, Unit& u) {
    const int L = round * 256 + c; if (L >= NUNITS) return false;
    const int xj = L % NXCD, off = L / NXCD;
    const int id = (off >= 26 && off < 32) ? 384 + xj * 6 + (off - 26) : xj * 48 + (off < 26 ? off : off - 6);
    if (id < 384) { u.seg = id >> 7; const int r = id & 127; u.pm = r & 7; u.pn = r >> 3;
        u.A = (u.seg == 2 ? P.xn : P.ckv) + (size_t)u.pm * BM * LDA;
        u.B = (u.seg == 0 ? P.wuk : (u.seg == 1 ? P.wuv : P.wq)) + u.pn * BM;
    } else { const int r = id - 384; u.seg = 3; u.pm = r & 7; u.pn = r >> 3;
        u.A = P.xn + (size_t)u.pm * BM * LDA; u.B = P.w1p + (u.pn < 5 ? u.pn * BM : 1120); }
    return true;
}
__device__ __forceinline__ u16 f2bf(float f) { return (u16)(cvt_pk_bf16(f, 0.f) & 0xffffu); }
__device__ __forceinline__ float gelu_tanh(float v) {
    const float u = 0.7978845608028654f * (v + 0.044715f * v * v * v);
    return v * __builtin_amdgcn_rcpf(1.0f + __expf(-2.0f * u));
}

__global__ __launch_bounds__(512, 2) void k_gemm1(Ptrs P) {
    extern __shared__ __attribute__((aligned(16))) unsigned char smem[];
    LAS unsigned char* lds = (LAS unsigned char*)smem;
    const int tid = threadIdx.x, wid = __builtin_amdgcn_readfirstlane(tid >> 6), lane = tid & 63, wr = wid >> 2, wc = wid & 3, fr = lane & 15, fq = lane >> 4;
    const int nt = 512 / BK;
    unsigned voffA[2], voffB[2];
#pragma unroll
    for (int i = 0; i < 2; ++i) {
        int R, C; stage_rc(tid * 16 + i * 8192, R, C); voffA[i] = (unsigned)(R * LDA + C) * 2u;
        const int row = 4 * wid + 32 * i + (lane >> 4), pos = lane & 15, X = (row & 3) | (((row >> 3) & 1) << 2);
        voffB[i] = (unsigned)(row * LDB + 8 * (pos ^ (X << 1))) * 2u;
    }
    const size_t kstepA = (size_t)BK * 2, kstepB = (size_t)BK * LDB * 2, hstepA = (size_t)HALF * LDA * 2, hstepB = (size_t)HALF * 2;
    const unsigned ldsw = (unsigned)wid * 1024u;
    const int aoff = lds_byte(wr * 64 + fr, fq * 8);
    int boffn[2];
    { const int g = lane >> 4, q = (lane & 15) >> 2, p = lane & 3, X = q | ((g & 1) << 2);
#pragma unroll
      for (int n = 0; n < 2; ++n) boffn[n] = 256 * (8 * g + q) + 32 * (((2 * wc) ^ X) ^ n) + 8 * p; }
    unsigned bbase[2];
#pragma unroll
    for (int n = 0; n < 2; ++n) bbase[n] = (unsigned)(uintptr_t)lds + 65536u + (unsigned)boffn[n];
#define SA(b, h) (((b) * 2 + (h)) * HTB)
#define SB(b, h) ((4 + (b) * 2 + (h)) * HTB)
#define STAGE(bufoff, gbase, voff) do { _Pragma("unroll") for (int _i = 0; _i < 2; ++_i) \
        __builtin_amdgcn_global_load_lds((const unsigned*)((const char*)(gbase) + (voff)[_i]), (LAS unsigned*)(lds + (bufoff) + ldsw + _i * 8192), 16, 0, 0); } while (0)
#define LDA_(dst, b, h) do { _Pragma("unroll") for (int m = 0; m < 4; ++m) _Pragma("unroll") for (int k = 0; k < 2; ++k) dst[m][k] = *(const LAS bf16x8*)(lds + SA(b, h) + aoff + m * 2048 + k * 1024); } while (0)
#define TRD(dst, base, off) asm volatile("ds_read_b64_tr_b16 %0, %1 offset:%2" : "=v"(dst) : "v"(base), "i"(off))
#define LDB_(dst, b, h) do { s16x4 l_[2][2], h_[2][2]; \
        _Pragma("unroll") for (int n = 0; n < 2; ++n) _Pragma("unroll") for (int k = 0; k < 2; ++k) { \
            TRD(l_[n][k], bbase[n], SB(b, h) - 65536 + k * 8192); TRD(h_[n][k], bbase[n], SB(b, h) - 65536 + k * 8192 + 1024); } \
        _Pragma("unroll") for (int n = 0; n < 2; ++n) _Pragma("unroll") for (int k = 0; k < 2; ++k) \
            dst[n][k] = (bf16x8){l_[n][k][0], l_[n][k][1], l_[n][k][2], l_[n][k][3], h_[n][k][0], h_[n][k][1], h_[n][k][2], h_[n][k][3]}; } while (0)
#define MMA(ai, bj, At, Bt) do { __builtin_amdgcn_s_setprio(1); _Pragma("unroll") for (int m = 0; m < 4; ++m) _Pragma("unroll") for (int n = 0; n < 2; ++n) _Pragma("unroll") for (int k = 0; k < 2; ++k) \
        acc[ai][bj][m][n] = __builtin_amdgcn_mfma_f32_16x16x32_bf16(Bt[n][k], At[m][k], acc[ai][bj][m][n], 0, 0, 0); __builtin_amdgcn_s_setprio(0); } while (0)
#define WAIT_V(n) asm volatile("s_waitcnt vmcnt(" #n ")" ::: "memory")
#define WAIT_L(n) asm volatile("s_waitcnt lgkmcnt(" #n ")" ::: "memory")
#define BAR __builtin_amdgcn_s_barrier()
#define SCHED __builtin_amdgcn_sched_barrier(0)
    Unit cur, nxt;
    if (!unit_of(P, 0, blockIdx.x, cur)) return;
    f32x4 acc[2][2][4][2];
#pragma unroll
    for (int a = 0; a < 2; ++a)
#pragma unroll
        for (int b = 0; b < 2; ++b)
#pragma unroll
            for (int m = 0; m < 4; ++m)
#pragma unroll
                for (int n = 0; n < 2; ++n) acc[a][b][m][n] = (f32x4){0.f, 0.f, 0.f, 0.f};
    bf16x8 At[4][2], B0[2][2], B1[2][2];
    const char* cA = (const char*)cur.A; const char* cB = (const char*)cur.B;
    STAGE(SB(0, 0), cB, voffB); STAGE(SA(0, 0), cA, voffA); STAGE(SB(0, 1), cB + hstepB, voffB); STAGE(SA(0, 1), cA + hstepA, voffA);
    if (wr == 1) BAR;
    WAIT_V(4); BAR;
    STAGE(SB(1, 0), cB + kstepB, voffB); STAGE(SA(1, 0), cA + kstepA, voffA); STAGE(SB(1, 1), cB + hstepB + kstepB, voffB);
    WAIT_V(6); BAR;
    int round = 0;
    for (;;) {
        const bool has_next = unit_of(P, round + 1, blockIdx.x, nxt);
        const char* nA = has_next ? (const char*)nxt.A : cA; const char* nB = has_next ? (const char*)nxt.B : cB;
        for (int t = 0; t < nt; t += 2) {
            const bool last = (t == nt - 2);
            const char* a1 = cA + (size_t)(t + 1) * kstepA;
            const char* a2 = last ? nA : cA + (size_t)(t + 2) * kstepA; const char* b2 = last ? nB : cB + (size_t)(t + 2) * kstepB;
            const char* a3 = a2 + kstepA; const char* b3 = b2 + kstepB;
            LDB_(B0, 0, 0); SCHED; LDA_(At, 0, 0); STAGE(SA(1, 1), a1 + hstepA, voffA);
            WAIT_L(8); BAR; WAIT_L(0); SCHED; MMA(0, 0, At, B0); BAR; SCHED;
            LDB_(B1, 0, 1); STAGE(SB(0, 0), b2, voffB);
            BAR; WAIT_L(0); SCHED; MMA(0, 1, At, B1); BAR;
            LDA_(At, 0, 1); STAGE(SA(0, 0), a2, voffA);
            BAR; WAIT_L(0); SCHED; MMA(1, 0, At, B0); BAR; SCHED;
            STAGE(SB(0, 1), b2 + hstepB, voffB);
            WAIT_V(6); BAR; MMA(1, 1, At, B1); BAR;
            LDB_(B0, 1, 0); SCHED; LDA_(At, 1, 0); STAGE(SA(0, 1), a2 + hstepA, voffA);
            WAIT_L(8); BAR; WAIT_L(0); SCHED; MMA(0, 0, At, B0); BAR; SCHED;
            LDB_(B1, 1, 1); STAGE(SB(1, 0), b3, voffB);
            BAR; WAIT_L(0); SCHED; MMA(0, 1, At, B1); BAR;
            LDA_(At, 1, 1); STAGE(SA(1, 0), a3, voffA);
            BAR; WAIT_L(0); SCHED; MMA(1, 0, At, B0); BAR; SCHED;
            STAGE(SB(1, 1), b3 + hstepB, voffB);
            WAIT_V(6); BAR; MMA(1, 1, At, B1); BAR;
        }
        {
            const int row0 = cur.pm * BM + wr * 64 + fr;
            if (cur.seg < 3 || ABL == 1) {
                u16* base = (cur.seg == 0 ? P.k : (cur.seg == 1 ? P.v : P.q));
#pragma unroll
                for (int bj = 0; bj < 2; ++bj) {
                    u16* hb = base + ((size_t)((cur.pn * 2 + bj) * 4 + wc) * S_LEN) * 32 + fq * 8;
#pragma unroll
                    for (int ai = 0; ai < 2; ++ai)
#pragma unroll
                        for (int m = 0; m < 4; ++m) {
                            const f32x4 v0 = acc[ai][bj][m][0], v1 = acc[ai][bj][m][1];
                            u32x4 w; w.x = cvt_pk_bf16(v0[0], v0[1]); w.y = cvt_pk_bf16(v0[2], v0[3]); w.z = cvt_pk_bf16(v1[0], v1[1]); w.w = cvt_pk_bf16(v1[2], v1[3]);
                            *(u32x4*)(hb + (size_t)(row0 + ai * HALF + m * 16) * 32) = w;
                        }
                }
            } else {
                const int n0 = cur.pn < 5 ? cur.pn * BM : 1120;
#pragma unroll
                for (int bj = 0; bj < 2; ++bj) {
                    const int cb = n0 + bj * HALF + wc * 32;
                    f32x4 bv[2];
#pragma unroll
                    for (int n = 0; n < 2; ++n) { const u32x2 braw = *(const u32x2*)(P.b1 + cb + 16 * n + 4 * fq);
                        bv[n] = (f32x4){__uint_as_float(braw.x << 16), __uint_as_float(braw.x & 0xffff0000u), __uint_as_float(braw.y << 16), __uint_as_float(braw.y & 0xffff0000u)}; }
                    u16* ob = P.ah + ((size_t)((NQKV + cb) >> 5) * S_LEN) * 32 + fq * 8;
#pragma unroll
                    for (int ai = 0; ai < 2; ++ai)
#pragma unroll
                        for (int m = 0; m < 4; ++m) {
                            f32x4 v0 = acc[ai][bj][m][0] + bv[0], v1 = acc[ai][bj][m][1] + bv[1];
#pragma unroll
                            for (int j = 0; j < 4; ++j) { v0[j] = gelu_tanh(v0[j]); v1[j] = gelu_tanh(v1[j]); }
                            u32x4 w; w.x = cvt_pk_bf16(v0[0], v0[1]); w.y = cvt_pk_bf16(v0[2], v0[3]); w.z = cvt_pk_bf16(v1[0], v1[1]); w.w = cvt_pk_bf16(v1[2], v1[3]);
                            *(u32x4*)(ob + (size_t)(row0 + ai * HALF + m * 16) * 32) = w;
                        }
                }
            }
        }
        if (!has_next) break;
#pragma unroll
        for (int a = 0; a < 2; ++a)
#pragma unroll
            for (int b = 0; b < 2; ++b)
#pragma unroll
                for (int m = 0; m < 4; ++m)
#pragma unroll
                    for (int n = 0; n < 2; ++n) acc[a][b][m][n] = (f32x4){0.f, 0.f, 0.f, 0.f};
        cur = nxt; cA = nA; cB = nB; ++round;
    }
    WAIT_V(0);
    if (wr == 0) BAR;
#undef SA
#undef SB
#undef STAGE
#undef LDA_
#undef TRD
#undef LDB_
#undef MMA
#undef WAIT_V
#undef WAIT_L
#undef BAR
#undef SCHED
}
}

namespace at {
constexpr int KVB = 64, SHM_T = KVB * HD * 2;
constexpr int LDS_Q = 5 * SHM_T, LDS_BYTES = LDS_Q + 8 * 8192;
constexpr float C2 = 0.08838834764831845f * 1.4426950408889634f;
__device__ __forceinline__ int v_st(int key, int d) { return ((key >> 3) * 4 + (d >> 5)) * 512 + (key & 7) * 64 + (d & 31) * 2; }
__device__ __forceinline__ float max3f(float a, float b, float c) { float r; asm("v_max3_f32 %0, %1, %2, %3" : "=v"(r) : "v"(a), "v"(b), "v"(c)); return r; }
__device__ __forceinline__ float max3p(float a, float b, float c) { float r; asm("s_nop 11\n\tv_max3_f32 %0, %1, %2, %3" : "=v"(r) : "v"(a), "v"(b), "v"(c)); return r; }
__device__ __forceinline__ unsigned pk2(float lo, float hi) { unsigned r; asm("v_cvt_pk_bf16_f32 %0, %1, %2" : "=v"(r) : "v"(lo), "v"(hi)); return r; }

__global__ __launch_bounds__(512, 1) void k_attn(const u16* __restrict__ Qb, const u16* __restrict__ Kb, const u16* __restrict__ Vb, u16* __restrict__ ah) {
    extern __shared__ __attribute__((aligned(16))) unsigned char smem[];
    LAS unsigned char* lds = (LAS unsigned char*)smem;
    const int tid = threadIdx.x, wid = __builtin_amdgcn_readfirstlane(tid >> 6), lane = tid & 63, r32 = lane & 31, hi = lane >> 5;
    const int b = blockIdx.x, h = (b & 7) + 8 * ((b >> 3) & 3),
        x = b >> 5;
    const int blk = (wid >> 2) ? 15 - x : x, R0 = 128 * blk + 32 * (wid & 3);
    const int tdiag = R0 >> 6, NTw = tdiag + 1, NT = 32 - 2 * x;
    const int srow = (tid >> 2) & 63, sq4 = tid & 3, sdb = tid >> 8;
    const size_t gofs0 = ((size_t)(h * 4 + sdb) * S_LEN + srow) * 32 + sq4 * 8, gd2 = (size_t)2 * S_LEN * 32;
    const u16* Kh = Kb + gofs0; const u16* Vh = Vb + gofs0;
    const int kw0 = (sdb * 4 + sq4) * 1024 + srow * 16, kw1 = kw0 + 8 * 1024;
    const int vw0 = 2 * SHM_T + v_st(srow, 32 * sdb + 8 * sq4), vw1 = 2 * SHM_T + v_st(srow, 32 * (sdb + 2) + 8 * sq4);
    const int ka0 = hi * 1024 + r32 * 16;
    const int qa0 = LDS_Q + wid * 8192 + hi * 512 + r32 * 16;
#pragma unroll
    for (int s = 0; s < 8; ++s) *(LAS bf16x8*)(lds + qa0 + 1024 * s) = *(const bf16x8*)(Qb + ((size_t)(h * 4 + (s >> 1)) * S_LEN + R0 + r32) * 32 + 16 * (s & 1) + 8 * hi);
    const int vb0 = 2 * SHM_T + 64 * (4 * hi + ((lane & 15) >> 2)) + 32 * ((lane >> 4) & 1) + 8 * (lane & 3);
    f32x16 o[4] = {};
    f32x16 a0, a1, b0, b1;
    bf16x8 pa0, pa1, pa2, pa3;
    float m_run = -1e30f, l_run = 0.f, al_prev = 1.f;
    bf16x8 sk0, sk1, sv0, sv1;
    sk0 = *(const bf16x8*)(Kh); sk1 = *(const bf16x8*)(Kh + gd2); sv0 = *(const bf16x8*)(Vh); sv1 = *(const bf16x8*)(Vh + gd2);
    *(LAS bf16x8*)(lds + kw0) = sk0; *(LAS bf16x8*)(lds + kw1) = sk1; *(LAS bf16x8*)(lds + vw0) = sv0; *(LAS bf16x8*)(lds + vw1) = sv1;
    __syncthreads();

#define SB do {} while (0)
#define KFRAG(kb, s, hf) (*(const LAS bf16x8*)(lds + (kb) + ka0 + 2048 * (s) + 512 * (hf)))
#define QFRAG(s) (*(const LAS bf16x8*)(lds + qa0 + 1024 * (s)))
#define VTR(vo, off) __builtin_amdgcn_ds_read_tr16_b64_v4i16((LAS s16x4*)(lds + vb0 + (vo) + (off)))
#define EXP2(v) __builtin_amdgcn_exp2f(v)
#define LDKV(tn) do { const u16* kp_ = Kh + (size_t)(tn) * KVB * 32; const u16* vp_ = Vh + (size_t)(tn) * KVB * 32;                     \
        sk0 = *(const bf16x8*)(kp_); sk1 = *(const bf16x8*)(kp_ + gd2); sv0 = *(const bf16x8*)(vp_); sv1 = *(const bf16x8*)(vp_ + gd2); } while (0)
#define WRKV(tn) do { const int kn_ = ((tn) & 1) * SHM_T, vn_ = ((tn) % 3) * SHM_T;                                                      \
        *(LAS bf16x8*)(lds + kn_ + kw0) = sk0; *(LAS bf16x8*)(lds + kn_ + kw1) = sk1;                                                     \
        *(LAS bf16x8*)(lds + vn_ + vw0) = sv0; *(LAS bf16x8*)(lds + vn_ + vw1) = sv1; } while (0)
#define MK8(w, i) (bf16x8)(u32x4){(w)[(i)], (w)[(i) + 1], (w)[(i) + 2], (w)[(i) + 3]}
#define MASK(X0, X1, t_) do { if ((t_) == tdiag) { const int dq = R0 + r32 - 64 * (t_) - 4 * hi;                                        \
        _Pragma("unroll") for (int r = 0; r < 16; ++r) { const int c = (r & 3) + 8 * (r >> 2);                                           \
            if (dq - c < 0) X0[r] = -__builtin_inff();                                                                                   \
            if (dq - c - 32 < 0) X1[r] = -__builtin_inff(); } } } while (0)
#define NEWMAX(pm, al, mnl) do { { auto rr_ = __builtin_amdgcn_permlane32_swap(__float_as_uint(pm), __float_as_uint(pm), false, false);   \
          pm = fmaxf(__uint_as_float(rr_[0]), __uint_as_float(rr_[1])); }                                                                 \
        if (__any(pm > m_run + THR_RAW)) { const float mn_ = fmaxf(m_run, pm); al = EXP2((m_run - mn_) * C2); m_run = mn_; } else al = 1.f; \
        mnl = -m_run * C2; } while (0)
#define RESCALE(al) do { if (__any(al < 1.f)) { _Pragma("unroll") for (int d = 0; d < 4; ++d) _Pragma("unroll") for (int r = 0; r < 16; ++r) o[d][r] *= al; } } while (0)
#define PV_PLAIN(vo) do { _Pragma("unroll") for (int S = 0; S < 4; ++S) {                                                                \
        const bf16x8 pf = S == 0 ? pa0 : (S == 1 ? pa1 : (S == 2 ? pa2 : pa3));                                                          \
        _Pragma("unroll") for (int d0 = 0; d0 < 4; ++d0) {                                                                               \
            const s16x4 lo = VTR(vo, 4096 * S + 512 * d0), hv = VTR(vo, 4096 * S + 512 * d0 + 2048);                                    \
            o[d0] = __builtin_amdgcn_mfma_f32_32x32x16_bf16((bf16x8){lo[0], lo[1], lo[2], lo[3], hv[0], hv[1], hv[2], hv[3]}, pf, o[d0], 0, 0, 0); } } } while (0)

#define STEADY(X0, X1, Y0, Y1, t) do { const int t_ = (t); const bool more = t_ + 1 < NT;                                                \
        if (more) LDKV(t_ + 1);                                                                                                           \
        const int kbo = (t_ & 1) * SHM_T, vbo = ((t_ + 2) % 3) * SHM_T;                                                                   \
        { const float mnl = -m_run * C2; float psA = 0.f, psB = 0.f; unsigned w0[8], w1[8];                                               \
          bf16x8 kl[8], kh[8], qf[8];                                                                                                     \
          kl[0] = KFRAG(kbo, 0, 0); kh[0] = KFRAG(kbo, 0, 1); qf[0] = QFRAG(0); kl[1] = KFRAG(kbo, 1, 0); kh[1] = KFRAG(kbo, 1, 1); qf[1] = QFRAG(1); SB; \
          _Pragma("unroll") for (int j = 0; j < 16; ++j) { const int s = j >> 1;                                                         \
              if ((j & 1) == 0) X0 = __builtin_amdgcn_mfma_f32_32x32x16_bf16(kl[s], qf[s], s == 0 ? (f32x16){} : X0, 0, 0, 0);           \
              else { X1 = __builtin_amdgcn_mfma_f32_32x32x16_bf16(kh[s], qf[s], s == 0 ? (f32x16){} : X1, 0, 0, 0);                      \
                     if (s + 2 < 8) { kl[s + 2] = KFRAG(kbo, s + 2, 0); kh[s + 2] = KFRAG(kbo, s + 2, 1); qf[s + 2] = QFRAG(s + 2); } }                            \
              Y1[j] = EXP2(fmaf(Y1[j], C2, mnl));                                                                                         \
              psA += Y0[j]; if (j >= 1) psB += Y1[j - 1];                                                                                 \
              if (j < 8) w0[j] = pk2(Y0[2 * j], Y0[2 * j + 1]); else w1[j - 8] = pk2(Y1[2 * (j - 8)], Y1[2 * (j - 8) + 1]);             \
              SB; }                                                                                                                       \
          psB += Y1[15]; l_run = l_run * al_prev + (psA + psB);                                                                           \
          pa0 = MK8(w0, 0); pa1 = MK8(w0, 4); pa2 = MK8(w1, 0); pa3 = MK8(w1, 4); }                                                      \
        MASK(X0, X1, t_);                                                                                                                 \
        { s16x4 vl[4], vh[4]; float mA, mB, pm, mnl2 = 0.f, al = 1.f;                                                                   \
          _Pragma("unroll") for (int d0 = 0; d0 < 4; ++d0) { vl[d0] = VTR(vbo, 512 * d0); vh[d0] = VTR(vbo, 512 * d0 + 2048); } SB;      \
          _Pragma("unroll") for (int j = 0; j < 16; ++j) { const int S = j >> 2, d0 = j & 3;                                             \
              const bf16x8 pf = S == 0 ? pa0 : (S == 1 ? pa1 : (S == 2 ? pa2 : pa3));                                                    \
              o[d0] = __builtin_amdgcn_mfma_f32_32x32x16_bf16((bf16x8){vl[d0][0], vl[d0][1], vl[d0][2], vl[d0][3],                       \
                                                                       vh[d0][0], vh[d0][1], vh[d0][2], vh[d0][3]}, pf, o[d0], 0, 0, 0);   \
              if (S < 3) { vl[d0] = VTR(vbo, 4096 * (S + 1) + 512 * d0); vh[d0] = VTR(vbo, 4096 * (S + 1) + 512 * d0 + 2048); }          \
              if (j == 0) { mA = max3p(X0[0], X0[1], X0[2]); mB = max3f(X1[0], X1[1], X1[2]); mA = max3f(mA, X0[3], X0[4]); mB = max3f(mB, X1[3], X1[4]); } \
              else if (j < 4) { _Pragma("unroll") for (int e = 0; e < 2; ++e) { const int i0 = 5 + 4 * (j - 1) + 2 * e, i1 = i0 + 1 < 16 ? i0 + 1 : i0; \
                                    mA = max3f(mA, X0[i0], X0[i1]); mB = max3f(mB, X1[i0], X1[i1]); } }                                  \
              else if (j == 4) { pm = fmaxf(mA, mB); NEWMAX(pm, al, mnl2); }                                                              \
              else if (j < 10) { const int i0 = 2 * (j - 5); X0[i0] = EXP2(fmaf(X0[i0], C2, mnl2)); X0[i0 + 1] = EXP2(fmaf(X0[i0 + 1], C2, mnl2)); } \
              else { const int i0 = j; X0[i0] = EXP2(fmaf(X0[i0], C2, mnl2)); }                                                          \
              SB; }                                                                                                                       \
          al_prev = al; RESCALE(al); }                                                                                                    \
        if (more) WRKV(t_ + 1);                                                                                                           \
        __syncthreads(); } while (0)
    constexpr float THR_RAW = 8.0f / 0.08838834764831845f;

#define DRAIN(Y0, Y1) do { const bool more = NTw + 1 < NT;                                                                                 \
        if (more) LDKV(NTw + 1);                                                                                                          \
        { const float mnl = -m_run * C2;                                                                                                  \
          _Pragma("unroll") for (int r = 0; r < 16; ++r) Y1[r] = EXP2(fmaf(Y1[r], C2, mnl));                                            \
          float ps = 0.f; _Pragma("unroll") for (int r = 0; r < 16; ++r) ps += Y0[r] + Y1[r];                                           \
          l_run = l_run * al_prev + ps; unsigned w0[8], w1[8];                                                                            \
          _Pragma("unroll") for (int i = 0; i < 8; ++i) { w0[i] = pk2(Y0[2 * i], Y0[2 * i + 1]); w1[i] = pk2(Y1[2 * i], Y1[2 * i + 1]); } \
          pa0 = MK8(w0, 0); pa1 = MK8(w0, 4); pa2 = MK8(w1, 0); pa3 = MK8(w1, 4); }                                                      \
        PV_PLAIN(((NTw + 2) % 3) * SHM_T);                                                                                                \
        { auto rr = __builtin_amdgcn_permlane32_swap(__float_as_uint(l_run), __float_as_uint(l_run), false, false);                     \
          const float inv = 1.0f / (__uint_as_float(rr[0]) + __uint_as_float(rr[1]));                                                    \
          int tz = threadIdx.x; asm volatile("" : "+v"(tz));                     \
          u16* orow = ah + ((size_t)(h * 4) * S_LEN + R0 + (tz & 31)) * 32 + 4 * ((tz >> 5) & 1);                                       \
          _Pragma("unroll") for (int d0 = 0; d0 < 4; ++d0) _Pragma("unroll") for (int g = 0; g < 4; ++g) {                             \
              const u32x2 w = {pk2(o[d0][4 * g] * inv, o[d0][4 * g + 1] * inv), pk2(o[d0][4 * g + 2] * inv, o[d0][4 * g + 3] * inv)};    \
              *(u32x2*)(orow + (size_t)d0 * S_LEN * 32 + 8 * g) = w; } }                                                                  \
        if (more) WRKV(NTw + 1);                                                                                                          \
        if (NTw < NT) __syncthreads(); } while (0)
#define DRAIN1(Y0, Y1) DRAIN(Y0, Y1)
    { const bool more = 1 < NT;
      if (more) LDKV(1);
      a0 = (f32x16){}; a1 = (f32x16){};
#pragma unroll
      for (int s = 0; s < 8; ++s) {
          const bf16x8 k0 = KFRAG(0, s, 0), k1 = KFRAG(0, s, 1), q0 = QFRAG(s);
          a0 = __builtin_amdgcn_mfma_f32_32x32x16_bf16(k0, q0, a0, 0, 0, 0);
          a1 = __builtin_amdgcn_mfma_f32_32x32x16_bf16(k1, q0, a1, 0, 0, 0); }
      MASK(a0, a1, 0);
      float pm = fmaxf(a0[0], a1[0]);
#pragma unroll
      for (int r = 1; r < 16; ++r) pm = fmaxf(pm, fmaxf(a0[r], a1[r]));
      float al, mnl; NEWMAX(pm, al, mnl); al_prev = al;
#pragma unroll
      for (int r = 0; r < 16; ++r) a0[r] = EXP2(fmaf(a0[r], C2, mnl));
      if (more) WRKV(1);
      __syncthreads(); }
    if (NTw > 1) {
        int t = 1;
        do {
            STEADY(b0, b1, a0, a1, t);
            if (t + 1 < NTw) STEADY(a0, a1, b0, b1, t + 1); else { a0 = b0; a1 = b1; }
            t += 2;
        } while (t < NTw);
    } else {
        DRAIN1(a0, a1);
    }
    if (NTw > 1) DRAIN(a0, a1);
    if (NTw + 2 < NT) {
        bf16x8 nk0, nk1, nv0, nv1;
        LDKV(NTw + 2);
        for (int t = NTw + 1; t < NT; ++t) {
            if (t + 2 < NT) { const u16* kp_ = Kh + (size_t)(t + 2) * KVB * 32; const u16* vp_ = Vh + (size_t)(t + 2) * KVB * 32;
                nk0 = *(const bf16x8*)(kp_); nk1 = *(const bf16x8*)(kp_ + gd2); nv0 = *(const bf16x8*)(vp_); nv1 = *(const bf16x8*)(vp_ + gd2); }
            if (t + 1 < NT) WRKV(t + 1);
            __syncthreads();
            sk0 = nk0; sk1 = nk1; sv0 = nv0; sv1 = nv1;
        }
    } else {
        for (int t = NTw + 1; t < NT; ++t) __syncthreads();
    }
#undef SB
#undef KFRAG
#undef QFRAG
#undef VTR
#undef EXP2
#undef LDKV
#undef WRKV
#undef MK8
#undef MASK
#undef NEWMAX
#undef RESCALE
#undef PV_PLAIN
#undef STEADY
#undef DRAIN
#undef DRAIN1
}
}

namespace g2 {
constexpr int STG = 32768, LDS_BYTES = 4 * STG;
__global__ __launch_bounds__(512, 2) void k_gemm2(const u16* __restrict__ ah, const u16* __restrict__ wo, const u16* __restrict__ w2, float* __restrict__ part) {
    extern __shared__ __attribute__((aligned(16))) unsigned char smem[];
    LAS unsigned char* lds = (LAS unsigned char*)smem;
    const int tid = threadIdx.x, wid = __builtin_amdgcn_readfirstlane(tid >> 6), lane = tid & 63, fr = lane & 15, fq = lane >> 4;
    const int b = blockIdx.x, xcd = b & 7, sp = xcd >> 1, pm = (xcd & 1) * 8 + ((b >> 3) & 7), pn = (b >> 6) & 3;
    const int kt0 = sp < 2 ? 22 * sp : 44 + 21 * (sp - 2), nkt = sp < 2 ? 22 : 21, nit = (nkt + 1) >> 1;
    const int grp = wid >> 2, qm = (wid >> 1) & 1, qn = wid & 1;
    unsigned voffA[2], voffB[2], voffBc;
#pragma unroll
    for (int i = 0; i < 2; ++i) {
        int R, C; g1::stage_rc(tid * 16 + i * 8192, R, C);
        voffA[i] = (unsigned)(((C >> 5) * S_LEN + R) * 32 + (C & 31)) * 2u;
        const int r = 4 * wid + 32 * i + (lane >> 4), pos = lane & 15, X = (r & 3) | (((r >> 3) & 1) << 2), ch = pos ^ (X << 1);
        const int p5 = r & 31, rr = (r & 32) | (16 * ((p5 >> 2) & 1) + 4 * (p5 >> 3) + (p5 & 3));
        voffB[i] = (unsigned)(rr * DM + 8 * ch) * 2u;
        if (i == 1) voffBc = (unsigned)(31 * DM + 8 * ch) * 2u;
    }
    const unsigned ldsw = (unsigned)wid * 1024u;
    const int aoff = g1::lds_byte(qm * 64 + fr, fq * 8);
    unsigned bbase[4];
    { const int g = lane >> 4, q = (lane & 15) >> 2, p = lane & 3, X = q | ((g & 1) << 2);
#pragma unroll
      for (int n = 0; n < 4; ++n) bbase[n] = (unsigned)(uintptr_t)lds + (unsigned)(256 * (8 * g + q) + 32 * (((4 * qn) ^ X) ^ n) + 8 * p); }
    const char* Ab = (const char*)ah + (size_t)pm * 128 * 64;
    const char* Bcol = (const char*)0 + (size_t)pn * 256;
#define G2_STAGE(kt, st) do { const int kt_ = (kt); \
        const char* a_ = Ab + (size_t)kt_ * (2 * S_LEN * 64); \
        const char* b_ = (kt_ < 64 ? (const char*)wo + (size_t)kt_ * 64 * DM * 2 : (const char*)w2 + (size_t)(kt_ - 64) * 64 * DM * 2) + (size_t)Bcol; \
        _Pragma("unroll") for (int _i = 0; _i < 2; ++_i) \
            __builtin_amdgcn_global_load_lds((const unsigned*)(a_ + voffA[_i]), (LAS unsigned*)(lds + (st) * STG + ldsw + _i * 8192), 16, 0, 0); \
        __builtin_amdgcn_global_load_lds((const unsigned*)(b_ + voffB[0]), (LAS unsigned*)(lds + (st) * STG + 16384 + ldsw), 16, 0, 0); \
        __builtin_amdgcn_global_load_lds((const unsigned*)(b_ + (kt_ == KT_TOT - 1 ? voffBc : voffB[1])), (LAS unsigned*)(lds + (st) * STG + 16384 + ldsw + 8192), 16, 0, 0); } while (0)
    f32x4 acc[4][4];
#pragma unroll
    for (int m = 0; m < 4; ++m)
#pragma unroll
        for (int n = 0; n < 4; ++n) acc[m][n] = (f32x4){0.f, 0.f, 0.f, 0.f};
    G2_STAGE(kt0, 0); if (nkt > 1) G2_STAGE(kt0 + 1, 1);
    for (int it = 0; it < nit; ++it) {
        asm volatile("s_waitcnt vmcnt(0)" ::: "memory"); __builtin_amdgcn_s_barrier();
        const int sb = (it & 1) * 2;
        if (it + 1 < nit) { const int kn = kt0 + 2 * (it + 1); G2_STAGE(kn, sb ^ 2); if (2 * (it + 1) + 1 < nkt) G2_STAGE(kn + 1, (sb ^ 2) + 1); }
        if (2 * it + grp < nkt) {
            const int st = sb + grp;
            bf16x8 At[4][2]; s16x4 bl[4][2], bh[4][2];
#pragma unroll
            for (int n = 0; n < 4; ++n)
#pragma unroll
                for (int k = 0; k < 2; ++k) {
                    asm volatile("ds_read_b64_tr_b16 %0, %1 offset:%2" : "=v"(bl[n][k]) : "v"(bbase[n] + (unsigned)(st * STG)), "i"(16384 + k * 8192));
                    asm volatile("ds_read_b64_tr_b16 %0, %1 offset:%2" : "=v"(bh[n][k]) : "v"(bbase[n] + (unsigned)(st * STG)), "i"(16384 + k * 8192 + 1024));
                }
#pragma unroll
            for (int m = 0; m < 4; ++m)
#pragma unroll
                for (int k = 0; k < 2; ++k) At[m][k] = *(const LAS bf16x8*)(lds + st * STG + aoff + m * 2048 + k * 1024);
            asm volatile("s_waitcnt lgkmcnt(0)" ::: "memory"); __builtin_amdgcn_sched_barrier(0);
#pragma unroll
            for (int k = 0; k < 2; ++k)
#pragma unroll
                for (int m = 0; m < 4; ++m)
#pragma unroll
                    for (int n = 0; n < 4; ++n) {
                        const bf16x8 Bf = (bf16x8){bl[n][k][0], bl[n][k][1], bl[n][k][2], bl[n][k][3], bh[n][k][0], bh[n][k][1], bh[n][k][2], bh[n][k][3]};
                        acc[m][n] = __builtin_amdgcn_mfma_f32_16x16x32_bf16(Bf, At[m][k], acc[m][n], 0, 0, 0);
                    }
        }
    }
    __syncthreads();
    LAS f32x4* red = (LAS f32x4*)lds + (wid & 3) * 1024 + lane;
    if (grp == 1) {
#pragma unroll
        for (int m = 0; m < 4; ++m)
#pragma unroll
            for (int n = 0; n < 4; ++n) red[(m * 4 + n) * 64] = acc[m][n];
    }
    __syncthreads();
    if (grp == 0) {
        float* dst = part + ((((size_t)sp * 16 + pm) * 4 + pn) * 4 + (wid & 3)) * 4096 + lane * 4;
#pragma unroll
        for (int m = 0; m < 4; ++m)
#pragma unroll
            for (int n = 0; n < 4; ++n) *(f32x4*)(dst + (m * 4 + n) * 256) = acc[m][n] + red[(m * 4 + n) * 64];
    }
#undef G2_STAGE
}
__global__ __launch_bounds__(256) void k_reduce(const float* __restrict__ part, const u16* __restrict__ x, const u16* __restrict__ b2, u16* __restrict__ out) {
    const int gt = blockIdx.x * 256 + threadIdx.x;
    const int lane = gt & 63, mn = (gt >> 6) & 15, quad = (gt >> 10) & 3, pn = (gt >> 12) & 3, pm = gt >> 14;
    const size_t off = ((((size_t)pm) * 4 + pn) * 4 + quad) * 4096 + mn * 256 + lane * 4, sstr = (size_t)16 * 4 * 4 * 4096;
    f32x4 a = *(const f32x4*)(part + off) + *(const f32x4*)(part + off + sstr) + *(const f32x4*)(part + off + 2 * sstr) + *(const f32x4*)(part + off + 3 * sstr);
    const int row = pm * 128 + (quad >> 1) * 64 + (mn >> 2) * 16 + (lane & 15), col = pn * 128 + (quad & 1) * 64 + (mn & 3) * 16 + (lane >> 4) * 4;
    const u32x2 xr = *(const u32x2*)(x + (size_t)row * DM + col), br = *(const u32x2*)(b2 + col);
    const float o0 = a[0] + __uint_as_float(xr.x << 16) + __uint_as_float(br.x << 16), o1 = a[1] + __uint_as_float(xr.x & 0xffff0000u) + __uint_as_float(br.x & 0xffff0000u);
    const float o2 = a[2] + __uint_as_float(xr.y << 16) + __uint_as_float(br.y << 16), o3 = a[3] + __uint_as_float(xr.y & 0xffff0000u) + __uint_as_float(br.y & 0xffff0000u);
    *(u32x2*)(out + (size_t)row * DM + col) = (u32x2){cvt_pk_bf16(o0, o1), cvt_pk_bf16(o2, o3)};
}
}

extern "C" void kernel_launch(void* const* d_in, const int* in_sizes, int n_in, void* d_out, int out_size, void* d_ws, size_t ws_size,
                              hipStream_t stream) {
    const u16* x = (const u16*)d_in[0]; const u16* ckv = (const u16*)d_in[1];
    const u16* wq = (const u16*)d_in[2]; const u16* wuk = (const u16*)d_in[3]; const u16* wuv = (const u16*)d_in[4]; const u16* wo = (const u16*)d_in[5];
    const u16* w1 = (const u16*)d_in[7]; const u16* b1 = (const u16*)d_in[8]; const u16* w2 = (const u16*)d_in[9]; const u16* b2 = (const u16*)d_in[10];
    char* ws = (char*)d_ws;
    u16* xn = (u16*)(ws + WS_XN); u16* w1p = (u16*)(ws + WS_W1P); u16* qb = (u16*)(ws + WS_Q); u16* kb = (u16*)(ws + WS_K); u16* vb = (u16*)(ws + WS_V); u16* ah = (u16*)(ws + WS_AH);
    static bool attr_done = false;
    if (!attr_done) { attr_done = true;
        (void)hipFuncSetAttribute((const void*)g1::k_gemm1, hipFuncAttributeMaxDynamicSharedMemorySize, g1::STAGE_BYTES); }
    k_prep<<<dim3((S_LEN + DM) / 4), dim3(256), 0, stream>>>(x, w1, xn, w1p, ah);
    g1::Ptrs P{xn, ckv, wq, wuk, wuv, w1p, b1, qb, kb, vb, ah};
    g1::k_gemm1<<<dim3(256), dim3(512), g1::STAGE_BYTES, stream>>>(P);
    (void)hipFuncSetAttribute((const void*)at::k_attn, hipFuncAttributeMaxDynamicSharedMemorySize, at::LDS_BYTES);
    at::k_attn<<<dim3(256), dim3(512), at::LDS_BYTES, stream>>>(qb, kb, vb, ah);
    float* part = (float*)(ws + WS_PART);
    (void)hipFuncSetAttribute((const void*)g2::k_gemm2, hipFuncAttributeMaxDynamicSharedMemorySize, g2::LDS_BYTES);
    g2::k_gemm2<<<dim3(256), dim3(512), g2::LDS_BYTES, stream>>>(ah, wo, w2, part);
    g2::k_reduce<<<dim3(S_LEN * DM / 4 / 256), dim3(256), 0, stream>>>(part, x, b2, (u16*)d_out);
}
```
